```python
import torch

def _softmax(x, axis=-1):
    return torch.softmax(x, dim=axis)
def _gelu(x):
    return torch.nn.functional.gelu(x, approximate="tanh")
def _silu(x):
    return torch.nn.functional.silu(x)
def _sigmoid(x):
    return torch.sigmoid(x)
def _layernorm(x, axis=-1, eps=1e-5):
    m = x.mean(dim=axis, keepdim=True)
    v = x.var(dim=axis, keepdim=True, unbiased=False)
    return (x - m) / torch.sqrt(v + eps)
def _scatter_add(tgt, idx, src, axis=0):
    return tgt.clone().index_add_(axis, idx, src)
def _segment_sum(src, seg, n):
    out = torch.zeros((n,) + src.shape[1:], dtype=src.dtype)
    return out.index_add_(0, seg, src)
def _pad(x, pw):
    flat = tuple(v for pair in reversed(pw) for v in pair)
    return torch.nn.functional.pad(x, flat)

kernel_name = 'gated_mix__gqa_block_cached__swiglu_ffn__decode'

def setup_inputs():

    _w = (lambda a: (lambda t: t.to(torch.bfloat16) if t.is_floating_point() else t)(torch.as_tensor(a)))
    def _A_setup():
        B, Sq, D = 2, 1, 576
        Hq, Hkv, Sk, Dh = 32, 16, 4096, 128
        assert Hq % Hkv == 0, (Hq, Hkv)
        return {
            "x": _w((lambda *s: torch.randn(*s, dtype=torch.bfloat16) * 0.05)(B, Sq, D)),
            "k_cache": _w((lambda *s: torch.randn(*s, dtype=torch.bfloat16) * 6.4)(B, Hkv, Sk, Dh)),
            "v_cache": _w((lambda *s: torch.randn(*s, dtype=torch.bfloat16) * 0.8)(B, Hkv, Sk, Dh)),
            "wq": _w((lambda *s: torch.randn(*s, dtype=torch.bfloat16) * 6.4)(D, Hq * Dh) * (D ** -0.5)),
            "wo": _w((lambda *s: torch.randn(*s, dtype=torch.bfloat16) * 0.8)(Hq * Dh, D) * ((Hq * Dh) ** -0.5)),
        }
    _A_in = _A_setup()

    def _B_setup():
        B, S, D, F = 2, 1, 576, 4608
        return {
            "x": _w((lambda *s: torch.randn(*s, dtype=torch.bfloat16) * 0.05)(B, S, D)),
            "w_gate": _w((lambda *s: torch.randn(*s, dtype=torch.bfloat16) * 0.2)(D, F)),
            "w_up": _w((lambda *s: torch.randn(*s, dtype=torch.bfloat16) * 0.2)(D, F)),
            "w_down": _w((lambda *s: torch.randn(*s, dtype=torch.bfloat16) * 0.2)(F, D)),
        }
    _B_in = _B_setup()

    return {
        "A_x": _A_in["x"],
        "A_k_cache": _A_in["k_cache"],
        "A_v_cache": _A_in["v_cache"],
        "A_wq": _A_in["wq"],
        "A_wo": _A_in["wo"],
        "B_x": _B_in["x"],
        "B_w_gate": _B_in["w_gate"],
        "B_w_up": _B_in["w_up"],
        "B_w_down": _B_in["w_down"],
        "gate_w": _w((lambda *s: torch.randn(*s, dtype=torch.bfloat16) * 0.05)(576) * 16.0),
    }

def reference(A_x, A_k_cache, A_v_cache, A_wq, A_wo, B_x, B_w_gate, B_w_up, B_w_down, gate_w):
    def _A_ref(x, k_cache, v_cache, wq, wo):
        B, Sq, D = x.shape
        Hq, Dh = 32, 128
        Hkv = k_cache.shape[1]
        rep = Hq // Hkv
        q = (lambda x, *d: x.permute(*d))((x @ wq).reshape(B, Sq, Hq, Dh), 0, 2, 1, 3)
        k = (lambda x, n, axis: torch.repeat_interleave(x, n, dim=axis))(k_cache, rep, axis=1)
        v = (lambda x, n, axis: torch.repeat_interleave(x, n, dim=axis))(v_cache, rep, axis=1)
        scores = torch.einsum("bhqd,bhkd->bhqk", q, k) * (Dh ** -0.5)
        p = _softmax(scores, axis=-1)
        o = torch.einsum("bhqk,bhkd->bhqd", p, v)
        return (lambda x, *d: x.permute(*d))(o, 0, 2, 1, 3).reshape(B, Sq, Hq * Dh) @ wo

    def _B_ref(x, w_gate, w_up, w_down):
        g = _silu(x @ w_gate)
        u = x @ w_up
        return (g * u) @ w_down

    x = A_x
    g = _sigmoid((x * gate_w).sum(-1, keepdims=True))
    a = _A_ref(x, A_k_cache, A_v_cache, A_wq, A_wo)
    b = _B_ref(x, B_w_gate, B_w_up, B_w_down)
    return g * a + (1 - g) * b
```

```cpp
#include <hip/hip_runtime.h>
#include <hip/hip_bf16.h>
#include <stdint.h>

typedef unsigned v4u __attribute__((ext_vector_type(4)));
typedef __bf16 bf16x2 __attribute__((ext_vector_type(2)));
typedef unsigned short u16;

constexpr int D = 576, HQ = 32, HKV = 16, SK = 4096, DH = 128, F = 4608, NQ = HQ * DH;
constexpr int NSPLIT = 8;

__device__ __forceinline__ float bflo(unsigned u) { return __builtin_bit_cast(float, u << 16); }
__device__ __forceinline__ float bfhi(unsigned u) { return __builtin_bit_cast(float, u & 0xffff0000u); }
__device__ __forceinline__ float bf2f(u16 v) { return __builtin_bit_cast(float, (unsigned)v << 16); }
__device__ __forceinline__ float rbf(float f) { return __bfloat162float(__float2bfloat16(f)); }

__global__ void __launch_bounds__(256) k1(const u16* x, const u16* wq, const u16* wg, const u16* wu, float* q, float* h) {
  __shared__ float xs[2][D];
  __shared__ float red[2][32][2][64];
  int tid = threadIdx.x;
  for (int i = tid; i < 2 * D; i += 256) xs[i / D][i % D] = bf2f(x[i]);
  __syncthreads();
  int blk = blockIdx.x;
  bool isq = blk < NQ / 64;
  int c = tid & 7, rg = tid >> 3;
  int nmat = isq ? 1 : 2;
  int col0 = isq ? blk * 64 : (blk - NQ / 64) * 64;
  int ld = isq ? NQ : F;
  for (int m = 0; m < nmat; ++m) {
    const u16* W = isq ? wq : (m == 0 ? wg : wu);
    float acc[2][8];
    for (int j = 0; j < 8; ++j) { acc[0][j] = 0.f; acc[1][j] = 0.f; }
#pragma unroll 6
    for (int k = rg; k < D; k += 32) {
      v4u w = *(const v4u*)(W + (size_t)k * ld + col0 + c * 8);
      float x0 = xs[0][k], x1 = xs[1][k];
#pragma unroll
      for (int j = 0; j < 4; ++j) {
        float a = bflo(w[j]), b = bfhi(w[j]);
        acc[0][2 * j] += x0 * a; acc[0][2 * j + 1] += x0 * b;
        acc[1][2 * j] += x1 * a; acc[1][2 * j + 1] += x1 * b;
      }
    }
    for (int j = 0; j < 8; ++j) { red[m][rg][0][c * 8 + j] = acc[0][j]; red[m][rg][1][c * 8 + j] = acc[1][j]; }
  }
  __syncthreads();
  if (tid < 128) {
    int b = tid >> 6, n = tid & 63;
    float s0 = 0.f, s1 = 0.f;
    for (int r = 0; r < 32; ++r) { s0 += red[0][r][b][n]; if (!isq) s1 += red[1][r][b][n]; }
    if (isq) q[b * NQ + col0 + n] = rbf(s0);
    else {
      float g = rbf(s0), u = rbf(s1);
      float sg = rbf(g / (1.f + __expf(-g)));
      h[b * F + col0 + n] = rbf(sg * u);
    }
  }
}

__global__ void __launch_bounds__(256) k2(const float* q, const u16* K, const u16* V, float* pml, float* po) {
  __shared__ float sc[2][512];
  __shared__ float red[4][2][4][128];
  __shared__ float wm[2][4];
  int s = blockIdx.x, kvh = blockIdx.y, b = blockIdx.z;
  int tid = threadIdx.x, lane = tid & 63, wave = tid >> 6, r = lane >> 4, c = lane & 15;
  const u16* Kb = K + ((size_t)(b * HKV + kvh) * SK + s * 512) * DH;
  const u16* Vb = V + ((size_t)(b * HKV + kvh) * SK + s * 512) * DH;
  float qf[2][8];
  for (int g = 0; g < 2; ++g) for (int j = 0; j < 8; ++j) qf[g][j] = q[b * NQ + (kvh * 2 + g) * DH + c * 8 + j];
  const float scale = 0.088388347648318440f;
#pragma unroll 8
  for (int i = 0; i < 32; ++i) {
    int key = wave * 128 + i * 4 + r;
    v4u k = __builtin_nontemporal_load((const v4u*)(Kb + (size_t)key * DH + c * 8));
    float d0 = 0.f, d1 = 0.f;
#pragma unroll
    for (int j = 0; j < 4; ++j) {
      float a = bflo(k[j]), bb = bfhi(k[j]);
      d0 += a * qf[0][2 * j] + bb * qf[0][2 * j + 1];
      d1 += a * qf[1][2 * j] + bb * qf[1][2 * j + 1];
    }
    for (int o = 1; o < 16; o <<= 1) { d0 += __shfl_xor(d0, o); d1 += __shfl_xor(d1, o); }
    if (c == 0) { sc[0][key] = d0 * scale; sc[1][key] = d1 * scale; }
  }
  __syncthreads();
  float m0 = fmaxf(sc[0][tid], sc[0][tid + 256]), m1 = fmaxf(sc[1][tid], sc[1][tid + 256]);
  for (int o = 1; o < 64; o <<= 1) { m0 = fmaxf(m0, __shfl_xor(m0, o)); m1 = fmaxf(m1, __shfl_xor(m1, o)); }
  if (lane == 0) { wm[0][wave] = m0; wm[1][wave] = m1; }
  __syncthreads();
  m0 = fmaxf(fmaxf(wm[0][0], wm[0][1]), fmaxf(wm[0][2], wm[0][3]));
  m1 = fmaxf(fmaxf(wm[1][0], wm[1][1]), fmaxf(wm[1][2], wm[1][3]));
  __syncthreads();
  float p00 = __expf(sc[0][tid] - m0), p01 = __expf(sc[0][tid + 256] - m0);
  float p10 = __expf(sc[1][tid] - m1), p11 = __expf(sc[1][tid + 256] - m1);
  sc[0][tid] = p00; sc[0][tid + 256] = p01; sc[1][tid] = p10; sc[1][tid + 256] = p11;
  float l0 = p00 + p01, l1 = p10 + p11;
  for (int o = 1; o < 64; o <<= 1) { l0 += __shfl_xor(l0, o); l1 += __shfl_xor(l1, o); }
  if (lane == 0) { wm[0][wave] = l0; wm[1][wave] = l1; }
  __syncthreads();
  l0 = wm[0][0] + wm[0][1] + wm[0][2] + wm[0][3];
  l1 = wm[1][0] + wm[1][1] + wm[1][2] + wm[1][3];
  float acc[2][8];
  for (int j = 0; j < 8; ++j) { acc[0][j] = 0.f; acc[1][j] = 0.f; }
#pragma unroll 8
  for (int i = 0; i < 32; ++i) {
    int key = wave * 128 + i * 4 + r;
    v4u v = __builtin_nontemporal_load((const v4u*)(Vb + (size_t)key * DH + c * 8));
    float p0 = sc[0][key], p1 = sc[1][key];
#pragma unroll
    for (int j = 0; j < 4; ++j) {
      float a = bflo(v[j]), bb = bfhi(v[j]);
      acc[0][2 * j] += p0 * a; acc[0][2 * j + 1] += p0 * bb;
      acc[1][2 * j] += p1 * a; acc[1][2 * j + 1] += p1 * bb;
    }
  }
  for (int j = 0; j < 8; ++j) { red[wave][0][r][c * 8 + j] = acc[0][j]; red[wave][1][r][c * 8 + j] = acc[1][j]; }
  __syncthreads();
  {
    int g = tid >> 7, d = tid & 127;
    float t = 0.f;
    for (int w = 0; w < 4; ++w) for (int rr = 0; rr < 4; ++rr) t += red[w][g][rr][d];
    int row = (b * HQ + kvh * 2 + g) * NSPLIT + s;
    po[(size_t)row * DH + d] = t;
    if (d == 0) { pml[row * 2] = g ? m1 : m0; pml[row * 2 + 1] = g ? l1 : l0; }
  }
}

__global__ void __launch_bounds__(288) k3(const float* pml, const float* po, const float* h, const u16* wo, const u16* wd,
                                          const u16* x, const u16* gw, float* part) {
  __shared__ float coef[2][32];
  __shared__ float red[4][2][D];
  __shared__ float gs[2];
  int tid = threadIdx.x, blk = blockIdx.x;
  bool iso = blk < NQ / 32;
  int row0 = iso ? blk * 32 : (blk - NQ / 32) * 32;
  if (tid < 128) {
    int b = tid >> 6, lane = tid & 63;
    float t = 0.f;
    for (int i = lane; i < D; i += 64) t += rbf(bf2f(x[b * D + i]) * bf2f(gw[i]));
    for (int o = 1; o < 64; o <<= 1) t += __shfl_xor(t, o);
    t = rbf(t);
    if (lane == 0) gs[b] = rbf(1.f / (1.f + __expf(-t)));
  }
  __syncthreads();
  if (tid < 64) {
    int b = tid >> 5, i = tid & 31;
    float v;
    if (iso) {
      int n = row0 + i, hd = n / DH, d = n % DH;
      int base = (b * HQ + hd) * NSPLIT;
      float M = -INFINITY;
      for (int s = 0; s < NSPLIT; ++s) M = fmaxf(M, pml[(base + s) * 2]);
      float L = 0.f, o = 0.f;
      for (int s = 0; s < NSPLIT; ++s) { float w = __expf(pml[(base + s) * 2] - M); L += w * pml[(base + s) * 2 + 1]; o += w * po[(size_t)(base + s) * DH + d]; }
      v = rbf(o / L) * gs[b];
    } else v = h[b * F + row0 + i] * (1.f - gs[b]);
    coef[b][i] = v;
  }
  __syncthreads();
  int c = tid % 72, rg = tid / 72;
  const u16* W = (iso ? wo : wd) + (size_t)row0 * D;
  float acc[2][8];
  for (int j = 0; j < 8; ++j) { acc[0][j] = 0.f; acc[1][j] = 0.f; }
#pragma unroll
  for (int i = 0; i < 8; ++i) {
    int r = rg * 8 + i;
    v4u w = *(const v4u*)(W + (size_t)r * D + c * 8);
    float c0 = coef[0][r], c1 = coef[1][r];
#pragma unroll
    for (int j = 0; j < 4; ++j) {
      float a = bflo(w[j]), b = bfhi(w[j]);
      acc[0][2 * j] += c0 * a; acc[0][2 * j + 1] += c0 * b;
      acc[1][2 * j] += c1 * a; acc[1][2 * j + 1] += c1 * b;
    }
  }
  for (int j = 0; j < 8; ++j) { red[rg][0][c * 8 + j] = acc[0][j]; red[rg][1][c * 8 + j] = acc[1][j]; }
  __syncthreads();
  for (int i = tid; i < 2 * D; i += 288) {
    int b = i / D, n = i % D;
    part[(size_t)blk * 2 * D + i] = red[0][b][n] + red[1][b][n] + red[2][b][n] + red[3][b][n];
  }
}

__global__ void __launch_bounds__(256) k4(const float* part, __hip_bfloat16* out) {
  __shared__ float red[16][16];
  int tid = threadIdx.x, o = tid & 15, g = tid >> 4;
  float t = 0.f;
#pragma unroll
  for (int i = 0; i < 17; ++i) t += part[(size_t)(g * 17 + i) * 2 * D + blockIdx.x * 16 + o];
  red[g][o] = t;
  __syncthreads();
  if (tid < 16) { float s = 0.f; for (int i = 0; i < 16; ++i) s += red[i][tid]; out[blockIdx.x * 16 + tid] = __float2bfloat16(s); }
}

extern "C" void kernel_launch(void* const* d_in, const int* in_sizes, int n_in, void* d_out, int out_size, void* d_ws, size_t ws_size,
                              hipStream_t stream) {
  const u16* x = (const u16*)d_in[0];
  const u16* K = (const u16*)d_in[1];
  const u16* V = (const u16*)d_in[2];
  const u16* wq = (const u16*)d_in[3];
  const u16* wo = (const u16*)d_in[4];
  const u16* wg = (const u16*)d_in[6];
  const u16* wu = (const u16*)d_in[7];
  const u16* wd = (const u16*)d_in[8];
  const u16* gw = (const u16*)d_in[9];
  float* ws = (float*)d_ws;
  float* q = ws;
  float* h = ws + 8192;
  float* pml = ws + 32768;
  float* po = ws + 65536;
  float* part = ws + 262144;
  k1<<<NQ / 64 + F / 64, 256, 0, stream>>>(x, wq, wg, wu, q, h);
  k2<<<dim3(NSPLIT, HKV, 2), 256, 0, stream>>>(q, K, V, pml, po);
  k3<<<272, 288, 0, stream>>>(pml, po, h, wo, wd, x, gw, part);
  k4<<<72, 256, 0, stream>>>(part, (__hip_bfloat16*)d_out);
}
```

```cpp
#include <hip/hip_runtime.h>
#include <stdint.h>

typedef unsigned v4u __attribute__((ext_vector_type(4)));
typedef unsigned v2u __attribute__((ext_vector_type(2)));
typedef float v4f __attribute__((ext_vector_type(4)));
typedef float v2f __attribute__((ext_vector_type(2)));
typedef __bf16 bf16x2 __attribute__((ext_vector_type(2)));
typedef __bf16 bf16x8 __attribute__((ext_vector_type(8)));
typedef unsigned short u16;
typedef __amdgpu_buffer_rsrc_t rsrc_t;

#define KV_AUX 2
#define WQ_AUX 2
#define W1_AUX 2
#define W2_AUX 2
#define WIN 6
#define WINB 6
#define WB_POS 23
#define QS 29
#define PVR 6
#define TD 520
#define TA 20
#define TDT 400
#define TPOST 3
#define TQ 500
#define TH 900
#define SPRIO 3
#define TG 300
#define TM 1300
#define FS (NS - 1)
#define OWAIT 10
#define PDEL 10

constexpr int D = 576, SK = 4096, DH = 128, F = 4608, NQ = 4096;
constexpr int AUX_SC1 = 16;
constexpr unsigned WS_Q = 4096;
constexpr unsigned WS_G = WS_Q + 32 * 8192;
constexpr unsigned WS_P = WS_G + 216 * 2048;
constexpr unsigned WS_M = WS_P + 16 * 16 * 16 * 128;
constexpr unsigned WS_T = WS_M + 256 * 128;
constexpr unsigned WS_O = WS_T + 256 * 128;
constexpr unsigned WS_END = WS_O + 36 * 32768;
constexpr unsigned SPIN_MAX = 1u << 18;
constexpr unsigned SPIN_LDS = 1u << 22;
constexpr unsigned OOB = 0x7ffff000u;

__device__ __forceinline__ float asf(unsigned u) { return __builtin_bit_cast(float, u); }
__device__ __forceinline__ unsigned asu(float f) { return __builtin_bit_cast(unsigned, f); }
__device__ __forceinline__ bf16x2 asb2(unsigned u) { return __builtin_bit_cast(bf16x2, u); }
__device__ __forceinline__ float bflo(unsigned u) { return __builtin_bit_cast(float, u << 16); }
__device__ __forceinline__ float bfhi(unsigned u) { return __builtin_bit_cast(float, u & 0xffff0000u); }
__device__ __forceinline__ unsigned f2bf(float f) {
  return (unsigned)__builtin_bit_cast(unsigned short, (__bf16)f);
}
__device__ __forceinline__ float rbf(float f) { return (float)(__bf16)f; }
__device__ __forceinline__ unsigned pack2(float lo, float hi) {
  bf16x2 v; v.x = (__bf16)lo; v.y = (__bf16)hi; return __builtin_bit_cast(unsigned, v);
}
__device__ __forceinline__ rsrc_t mk(const void* p, unsigned bytes) {
  return __builtin_amdgcn_make_buffer_rsrc((void*)p, (short)0, (int)bytes, 0x00020000);
}
template <int AUX> __device__ __forceinline__ v4u ld16(rsrc_t r, unsigned vo, unsigned so) {
  return __builtin_amdgcn_raw_buffer_load_b128(r, (int)vo, (int)so, AUX);
}
__device__ __forceinline__ void st16(rsrc_t r, unsigned vo, v4u v) { __builtin_amdgcn_raw_buffer_store_b128(v, r, (int)vo, 0, AUX_SC1); }

template <int CTRL> __device__ __forceinline__ float dpp(float v) {
  return __builtin_bit_cast(float, __builtin_amdgcn_update_dpp(0, __builtin_bit_cast(int, v), CTRL, 0xf, 0xf, true));
}
constexpr int DPP_X1 = 0xB1, DPP_X2 = 0x4E, DPP_R4 = 0x124, DPP_R8 = 0x128;
template <int CTRL> __device__ __forceinline__ float tstep(bool sel, float a, float b) {
  float keep = sel ? b : a, send = sel ? a : b;
  return keep + dpp<CTRL>(send);
}
__device__ __forceinline__ float sw32_add(float a, float b) {
  auto r = __builtin_amdgcn_permlane32_swap(__builtin_bit_cast(unsigned, a), __builtin_bit_cast(unsigned, b), false, false);
  return __builtin_bit_cast(float, (unsigned)r[0]) + __builtin_bit_cast(float, (unsigned)r[1]);
}
__device__ __forceinline__ float sw16_add(float a, float b) {
  auto r = __builtin_amdgcn_permlane16_swap(__builtin_bit_cast(unsigned, a), __builtin_bit_cast(unsigned, b), false, false);
  return __builtin_bit_cast(float, (unsigned)r[0]) + __builtin_bit_cast(float, (unsigned)r[1]);
}
__device__ __forceinline__ float sw32_max(float a) {
  auto r = __builtin_amdgcn_permlane32_swap(__builtin_bit_cast(unsigned, a), __builtin_bit_cast(unsigned, a), false, false);
  return fmaxf(__builtin_bit_cast(float, (unsigned)r[0]), __builtin_bit_cast(float, (unsigned)r[1]));
}
__device__ __forceinline__ float sw16_max(float a) {
  auto r = __builtin_amdgcn_permlane16_swap(__builtin_bit_cast(unsigned, a), __builtin_bit_cast(unsigned, a), false, false);
  return fmaxf(__builtin_bit_cast(float, (unsigned)r[0]), __builtin_bit_cast(float, (unsigned)r[1]));
}
__device__ __forceinline__ float wave_sum(float v) {
  v += dpp<DPP_X1>(v); v += dpp<DPP_X2>(v); v += dpp<DPP_R4>(v); v += dpp<DPP_R8>(v);
  v = sw16_add(v, v); return sw32_add(v, v);
}
__device__ __forceinline__ void fma8(v2f (&acc)[8], v4u w, v2f xp) {
#pragma unroll
  for (int j = 0; j < 4; ++j) {
    float a = bflo(w[j]), b = bfhi(w[j]);
    acc[2 * j] = __builtin_elementwise_fma((v2f){a, a}, xp, acc[2 * j]);
    acc[2 * j + 1] = __builtin_elementwise_fma((v2f){b, b}, xp, acc[2 * j + 1]);
  }
}

template <int V> struct ic { static constexpr int value = V; };
template <int A, int B, class Fn> __device__ __forceinline__ void static_for(Fn&& f) {
  if constexpr (A < B) { f(ic<A>{}); static_for<A + 1, B>(f); }
}
#define STR2(x) #x
#define STR(x) STR2(x)
#define SB() __builtin_amdgcn_sched_barrier(0)
#define FENCE() do { SB(); asm volatile("" ::: "memory"); SB(); } while (0)
#define BAR() do { SB(); asm volatile("s_waitcnt lgkmcnt(0)\n\ts_barrier" ::: "memory"); SB(); } while (0)
template <int N> __device__ __forceinline__ void pace() {
  SB(); asm volatile("s_waitcnt vmcnt(%0)" ::"n"(N) : "memory"); SB();
}

__device__ __forceinline__ v4f mfma(v4u a, v4u b, v4f c) {
  return __builtin_amdgcn_mfma_f32_16x16x32_bf16(__builtin_bit_cast(bf16x8, a), __builtin_bit_cast(bf16x8, b), c, 0, 0, 0);
}
__device__ __forceinline__ v4u andm(unsigned v, v4u m) { return (v4u){v & m[0], v & m[1], v & m[2], v & m[3]}; }
__device__ __forceinline__ unsigned lds_add(unsigned* cnt, int lane) {
  FENCE();
  unsigned old = 0;
  if (lane == 0) old = __hip_atomic_fetch_add(cnt, 1u, __ATOMIC_RELAXED, __HIP_MEMORY_SCOPE_WORKGROUP);
  FENCE();
  return old;
}
__device__ __forceinline__ unsigned rank_of(unsigned old) {
  const unsigned l = (unsigned)__builtin_amdgcn_readfirstlane((int)old);
  FENCE();
  return l;
}
template <int N> __device__ __forceinline__ unsigned poll(rsrc_t rws, const unsigned (&off)[N], v4u (&cur)[N], unsigned tag) {
  bool bad[N], any;
  unsigned rounds = 0;
  auto check = [&]() {
    any = false;
#pragma unroll
    for (int k = 0; k < N; ++k) { bad[k] = off[k] != OOB && (cur[k][1] != tag || cur[k][3] != tag); any |= bad[k]; }
    return __builtin_amdgcn_ballot_w64(any) != 0ull;
  };
  auto issue = [&](v4u (&d)[N]) {
    FENCE();
#pragma unroll
    for (int k = 0; k < N; ++k) d[k] = ld16<AUX_SC1>(rws, bad[k] ? off[k] : OOB, 0);
    FENCE();
  };
  auto merge = [&](const v4u (&d)[N]) {
#pragma unroll
    for (int k = 0; k < N; ++k) cur[k] = bad[k] ? d[k] : cur[k];
  };
  if (check()) {
    v4u A[N];
#pragma nounroll
    for (unsigned spin = 0; spin < SPIN_MAX; ++spin) {
      __builtin_amdgcn_s_sleep(1);
      issue(A);
      merge(A);
      ++rounds;
      if (!check()) break;
    }
  }
  FENCE();
  return rounds;
}

#define PA 144
#define PB 156
#define P2_LD(R, k) "buffer_load_dwordx4 v[" STR(R) "+4*" #k ":" STR(R) "+4*" #k "+3], %[o" #k "], %[rs], 0 offen sc1\n\t"
#define P2_CK(R, k) "v_cmpx_eq_u32_e32 vcc, %[tg], v[" STR(R) "+4*" #k "+1]\n\tv_cmpx_eq_u32_e32 vcc, %[tg], v[" STR(R) "+4*" #k "+3]\n\t"
#define P2_MV(R, k) "v_mov_b32 %[a" #k "], v[" STR(R) "+4*" #k "]\n\tv_mov_b32 %[b" #k "], v[" STR(R) "+4*" #k "+2]\n\t"
#define P2_ALL2(M, R) M(R, 0) M(R, 1)
#define P2_ALL3(M, R) M(R, 0) M(R, 1) M(R, 2)
#define P2_STEP(ALL, R, N)                                                                   \
  "s_waitcnt vmcnt(" #N ")\n\t" ALL(P2_CK, R) ALL(P2_MV, R)                                  \
  "s_andn2_b64 %[bad], %[bad], exec\n\ts_cbranch_scc0 2f\n\ts_mov_b64 exec, %[bad]\n\t" ALL(P2_LD, R)
#define P2_BODY(ALL, N)                                                                      \
  "s_mov_b64 %[sv], exec\n\ts_mov_b64 exec, %[bad]\n\t" ALL(P2_LD, PA) "s_sleep " STR(PDEL) "\n\t" ALL(P2_LD, PB) \
  "1:\n\t" P2_STEP(ALL, PA, N) P2_STEP(ALL, PB, N)                                         \
  "s_sub_u32 %[n], %[n], 1\n\ts_cbranch_scc0 1b\n\t2:\n\ts_mov_b64 exec, %[sv]"
#define P2_CLOB "vcc", "scc", "memory", "v144", "v145", "v146", "v147", "v148", "v149", "v150", "v151", "v152", "v153", "v154", "v155", "v156", "v157", "v158", "v159", "v160", "v161", "v162", "v163", "v164", "v165", "v166", "v167"
template <int N> __device__ __forceinline__ void poll2(v4u rs, const unsigned (&off)[N], const v4u (&cur)[N], unsigned (&val)[2 * N], unsigned tag) {
  bool any = false;
#pragma unroll
  for (int k = 0; k < N; ++k) { any |= cur[k][1] != tag || cur[k][3] != tag; val[2 * k] = cur[k][0]; val[2 * k + 1] = cur[k][2]; }
  unsigned long long bad = __builtin_amdgcn_ballot_w64(any && off[0] != OOB), sv;
  if (bad != 0ull) {
    unsigned n = SPIN_MAX;
    SB();
    if constexpr (N == 3)
      asm volatile(P2_BODY(P2_ALL3, 3)
                   : [bad] "+s"(bad), [sv] "=&s"(sv), [n] "+s"(n), [a0] "+v"(val[0]), [b0] "+v"(val[1]), [a1] "+v"(val[2]), [b1] "+v"(val[3]),
                     [a2] "+v"(val[4 % (2 * N)]), [b2] "+v"(val[5 % (2 * N)])
                   : [o0] "v"(off[0]), [o1] "v"(off[1]), [o2] "v"(off[2 % N]), [rs] "s"(rs), [tg] "s"(tag)
                   : P2_CLOB);
    else
      asm volatile(P2_BODY(P2_ALL2, 2)
                   : [bad] "+s"(bad), [sv] "=&s"(sv), [n] "+s"(n), [a0] "+v"(val[0]), [b0] "+v"(val[1]), [a1] "+v"(val[2]), [b1] "+v"(val[3])
                   : [o0] "v"(off[0]), [o1] "v"(off[1]), [rs] "s"(rs), [tg] "s"(tag)
                   : P2_CLOB);
    SB();
  }
  FENCE();
}
__device__ __forceinline__ unsigned duplo(unsigned a) { return __builtin_amdgcn_perm(a, a, 0x01000100u); }
__device__ __forceinline__ unsigned duphi(unsigned a) { return __builtin_amdgcn_perm(a, a, 0x03020302u); }
typedef unsigned v3u __attribute__((ext_vector_type(3)));

__device__ __forceinline__ void lds_inc(unsigned* cnt, int lane) {
  FENCE();
  if (lane == 0) (void)__hip_atomic_fetch_add(cnt, 1u, __ATOMIC_RELAXED, __HIP_MEMORY_SCOPE_WORKGROUP);
  FENCE();
}
__device__ __forceinline__ void lds_wait(unsigned* cnt, unsigned v) {
#pragma nounroll
  for (unsigned spin = 0; spin < SPIN_LDS; ++spin) {
    FENCE();
    if ((unsigned)__builtin_amdgcn_readfirstlane((int)__hip_atomic_load(cnt, __ATOMIC_RELAXED, __HIP_MEMORY_SCOPE_WORKGROUP)) == v) break;
    __builtin_amdgcn_s_sleep(1);
  }
  FENCE();
}
__device__ __forceinline__ void twait(unsigned ref, unsigned ticks) {
  FENCE();
#pragma nounroll
  for (int spin = 0; spin < 4096; ++spin) {
    if ((unsigned)__builtin_amdgcn_s_memrealtime() - ref >= ticks) break;
    __builtin_amdgcn_s_sleep(2);
  }
  FENCE();
}
constexpr int clampi(int v, int lo, int hi) { return v < lo ? lo : v > hi ? hi : v; }
constexpr int NS = 43 - TPOST;
static_assert(FS >= QS + 5 && FS < (QS + 6 > NS ? QS + 6 : NS), "FS");
constexpr int pv_done(int n) { return n < QS + 6 ? 0 : clampi(clampi(n - 21 - WINB, 0, 16), 0, (n - QS - 5) * PVR); }

__global__ void __launch_bounds__(576, 3)
fused_decode(const u16* x, const u16* Kc, const u16* Vc, const u16* wq,
             const u16* wo, const u16* wg, const u16* wu, const u16* wd,
             const u16* gw, u16* out, unsigned char* ws) {
  __shared__ __attribute__((aligned(16))) v2f qpart[8][64];
  __shared__ __attribute__((aligned(16))) unsigned qs[128];
  __shared__ __attribute__((aligned(16))) v4f gured[8][64];
  __shared__ __attribute__((aligned(16))) v4f dred[8][64];
  __shared__ __attribute__((aligned(16))) float mls[2][2][8];
  __shared__ __attribute__((aligned(16))) unsigned cf[2][4][12];
  __shared__ __attribute__((aligned(16))) v2f gs[2];
  __shared__ __attribute__((aligned(16))) unsigned cnt[12];
  __shared__ __attribute__((aligned(16))) float opart[16][8];

  const int tid = threadIdx.x, lane = tid & 63;
  const int wave = __builtin_amdgcn_readfirstlane(tid >> 6);
  const int bid = blockIdx.x;
  const int g = bid;
  const int kvh = g >> 4, i16 = g & 15, b = i16 >> 3, s = i16 & 7;
  const int c = lane & 15, r = lane >> 4;
  unsigned tref = (unsigned)__builtin_amdgcn_s_memrealtime();
  SB();
  unsigned tprev = tref, tlds = tref;

  const rsrc_t rws = mk(ws, WS_END);
  const v4u rwsv = {(unsigned)(uintptr_t)ws, (unsigned)((uintptr_t)ws >> 32) & 0xffffu, WS_END, 0x00020000u};
  const rsrc_t rx = mk(x, 2 * D * 2);
  rsrc_t rwo, rwd;
  unsigned w2o;
  v4u T[5];
#define TLOAD(k) T[k] = (k) < 2 ? ld16<W2_AUX>(rwo, w2o, (k) * 9216) : ld16<W2_AUX>(rwd, w2o, ((k) - 2) * 9216)
  v4u msk;
  auto common = [&]() {
    int cc = c, gg = g;
    asm volatile("" : "+v"(cc), "+s"(gg));
    rwo = mk(wo + (size_t)gg * 16 * D, 16 * D * 2);
    rwd = mk(wd + (size_t)gg * 18 * D, 18 * D * 2);
    w2o = (unsigned)(((cc >> 3) * 4 + r) * 1152 + wave * 128 + (cc & 7) * 16);
    const unsigned hm = (cc & 1) ? 0xffff0000u : 0x0000ffffu;
    const int tj = (cc >> 3) * 2 + ((cc >> 1) & 1);
#pragma unroll
    for (int t = 0; t < 4; ++t) msk[t] = (tj == t) ? hm : 0u;
  };
  unsigned tag;

  if (wave == 8) {
    __builtin_amdgcn_s_setprio(SPRIO);
    if (lane < 24) ((v4u*)cf)[lane] = (v4u){0u, 0u, 0u, 0u};
    else if (lane < 27) ((v4u*)cnt)[lane - 24] = (v4u){lane == 26 ? tref : 0u, 0u, 0u, 0u};
    FENCE();
    const unsigned E = __builtin_amdgcn_raw_buffer_load_b32(rws, 0, 0, AUX_SC1);
    const rsrc_t rgw = mk(gw, D * 2);
    const unsigned o1 = lane < 8 ? (unsigned)(1024 + lane * 16) : OOB;
    const v4u xa[2] = {ld16<0>(rx, lane * 16, 0), ld16<0>(rx, o1, 0)};
    const v4u xb[2] = {ld16<0>(rx, lane * 16, D * 2), ld16<0>(rx, o1, D * 2)};
    const v4u gv[2] = {ld16<0>(rgw, lane * 16, 0), ld16<0>(rgw, o1, 0)};
    FENCE();
    BAR();
    common();
    tag = (unsigned)__builtin_amdgcn_readfirstlane((int)E) + 1u;
    if (lane == 0) cnt[6] = tag;
    st16(rws, lane == 0 ? WS_T + (unsigned)(g * 128) : OOB, (v4u){tref, tag, tref, tag});
    {
      float t0 = 0.f, t1 = 0.f;
#pragma unroll
      for (int k = 0; k < 2; ++k)
#pragma unroll
        for (int j = 0; j < 4; ++j) {
          const unsigned a = xa[k][j], bq = xb[k][j], gq = gv[k][j];
          t0 += rbf(bflo(a) * bflo(gq)) + rbf(bfhi(a) * bfhi(gq));
          t1 += rbf(bflo(bq) * bflo(gq)) + rbf(bfhi(bq) * bfhi(gq));
        }
      t0 = rbf(wave_sum(t0)); t1 = rbf(wave_sum(t1));
      const float tb = lane ? t1 : t0;
      const float g0 = rbf(1.f / (1.f + __expf(-tb)));
      if (lane < 2) gs[lane] = (v2f){g0, rbf(1.f - g0)};
    }
    lds_wait(&cnt[0], 8u);
    {
      v2f qv = {0.f, 0.f};
#pragma unroll
      for (int w = 0; w < 8; ++w) qv += qpart[w][lane];
      const int pair = (i16 & 3) * 32 + (lane & 7) * 4 + ((lane >> 4) & 1) * 2 + ((lane >> 3) & 1);
      st16(rws, WS_Q + (unsigned)((kvh * 2 + (lane >> 5)) * 8192 + (i16 >> 2) * 2048 + pair * 16), (v4u){asu(qv.x), tag, asu(qv.y), tag});
    }
    FENCE();
    twait(tref, TG);
    const v4u tv = ld16<AUX_SC1>(rws, WS_T + (unsigned)((kvh * 16 + c) * 128), 0);
    FENCE();
    static_for<0, 5>([&](auto k) { TLOAD(k.value); });
    FENCE();
    {
      int d = (tv[1] == tag) ? (int)(tv[0] - tref) : 0;
      d = d < -200 ? 0 : d;
      d = min(d, __builtin_amdgcn_update_dpp(0, d, DPP_X1, 0xf, 0xf, true));
      d = min(d, __builtin_amdgcn_update_dpp(0, d, DPP_X2, 0xf, 0xf, true));
      d = min(d, __builtin_amdgcn_update_dpp(0, d, DPP_R4, 0xf, 0xf, true));
      d = min(d, __builtin_amdgcn_update_dpp(0, d, DPP_R8, 0xf, 0xf, true));
      tref += (unsigned)__builtin_amdgcn_readfirstlane(d);
      if (lane == 0) cnt[8] = tref;
      FENCE();
    }
    twait(tref, TQ);
    unsigned qoff[8];
    v4u qg[8];
#pragma unroll
    for (int k = 0; k < 8; ++k) {
      qoff[k] = WS_Q + (unsigned)((kvh * 2 + b) * 8192 + (k & 3) * 2048 + (k >> 2) * 1024 + lane * 16);
      qg[k] = ld16<AUX_SC1>(rws, qoff[k], 0);
    }
    FENCE();
    lds_wait(&cnt[1], 8u);
    {
      v4f v = gured[0][lane];
#pragma unroll
      for (int w = 1; w < 8; ++w) v += gured[w][lane];
      const unsigned ga = bid < 216 ? WS_G + (unsigned)(bid * 2048 + (r & 1) * 1024 + (c * 2 + (r >> 1)) * 16) : OOB;
      st16(rws, ga, (v4u){asu(v[0]), tag, asu(v[1]), tag});
      st16(rws, ga + 512u, (v4u){asu(v[2]), tag, asu(v[3]), tag});
    }
    FENCE();
    {
      poll<8>(rws, qoff, qg, tag);
#pragma unroll
      for (int h = 0; h < 2; ++h) {
        const float q0 = (asf(qg[4 * h][0]) + asf(qg[4 * h + 1][0])) + (asf(qg[4 * h + 2][0]) + asf(qg[4 * h + 3][0]));
        const float q1 = (asf(qg[4 * h][2]) + asf(qg[4 * h + 1][2])) + (asf(qg[4 * h + 2][2]) + asf(qg[4 * h + 3][2]));
        qs[h * 64 + lane] = pack2(q0, q1);
      }
      lds_inc(&cnt[2], lane);
    }
    twait(tref, TH);
    {
      unsigned hoff[3];
      v4u hg[3];
      const int l5 = lane & 31, bb = l5 >= 9, j9 = l5 - bb * 9;
      const unsigned p = (unsigned)(g * 9 + j9);
      const unsigned ga = WS_G + (((unsigned)(lane >> 5) * 36u + (p >> 6)) * 3u) * 2048u + (unsigned)bb * 1024u + (p & 1u) * 512u + ((p & 63u) >> 1) * 16u;
#pragma unroll
      for (int k = 0; k < 3; ++k) {
        hoff[k] = l5 < 18 ? ga + (unsigned)k * 2048u : OOB;
        hg[k] = ld16<AUX_SC1>(rws, hoff[k], 0);
      }
      poll<3>(rws, hoff, hg, tag);
      const float s0 = (asf(hg[0][0]) + asf(hg[1][0])) + asf(hg[2][0]), s1 = (asf(hg[0][2]) + asf(hg[1][2])) + asf(hg[2][2]);
      auto sw = __builtin_amdgcn_permlane32_swap(asu(s0), asu(s1), false, false);
      const float Gv = rbf(asf((unsigned)sw[0])), Uv = rbf(asf((unsigned)sw[1]));
      const float sg = rbf(Gv / (1.f + __expf(-Gv)));
      const unsigned hb = f2bf(sg * Uv);
      const int row = j9 * 2 + (lane >> 5);
      if (l5 < 18) cf[bb][row & 3][4 + (row >> 2)] = hb | (hb << 16);
      lds_inc(&cnt[5], lane);
    }
    lds_wait(&cnt[7], 8u);
    {
      const int hh = lane & 1;
      const v4f ma = *(const v4f*)&mls[0][hh][0], mb = *(const v4f*)&mls[0][hh][4];
      const v4f la = *(const v4f*)&mls[1][hh][0], lb = *(const v4f*)&mls[1][hh][4];
      const float M = fmaxf(fmaxf(fmaxf(ma[0], ma[1]), fmaxf(ma[2], ma[3])), fmaxf(fmaxf(mb[0], mb[1]), fmaxf(mb[2], mb[3])));
      float L = 0.f;
#pragma unroll
      for (int w = 0; w < 4; ++w) L += __builtin_amdgcn_exp2f(ma[w] - M) * la[w] + __builtin_amdgcn_exp2f(mb[w] - M) * lb[w];
      st16(rws, lane < 2 ? WS_M + (unsigned)(g * 128 + lane * 16) : OOB, (v4u){asu(M), tag, asu(L), tag});
    }
  } else {
    const size_t kvoff = ((size_t)(b * 16 + kvh) * SK + s * 512) * DH;
    const rsrc_t rK = mk(Kc + kvoff, 512 * 256), rV = mk(Vc + kvoff, 512 * 256);
    const int wv = wave;
    const unsigned ko = (unsigned)(wv * 16384 + ((unsigned)c & 7u) * 256 + ((c >> 3) * 4 + r) * 16);
#define KSO(ix) ((unsigned)(((ix) >> 1) * 2048 + ((ix) & 1) * 128))
    v4u Kr[16], Vr[16], G[6];
    int bd = bid, td = tid;
    SB();
#pragma unroll
    for (int k = 0; k < 3; ++k) Kr[k] = ld16<KV_AUX>(rK, ko, KSO(k));
    SB();
    asm volatile("" : "+s"(bd), "+v"(td));
    SB();
    const int qrow0 = ((bd & 15) >> 2) * 144;
    const rsrc_t rq = mk(wq + (size_t)qrow0 * NQ, 144 * NQ * 2);
    const int gcol = bd / 3, gth = bd - gcol * 3, gm = gcol >= 36, gcs = gcol - gm * 36;
    const rsrc_t rgu = mk((gm ? wu : wg) + (bd < 216 ? (size_t)gth * 192 * F : 0), bd < 216 ? 192 * F * 2 : 0);
    const unsigned vo = (unsigned)(wv * 16384 + (unsigned)r * 1024 + c * 16);
#define VSO(ix) ((unsigned)(((ix) >> 2) * 4096 + ((ix) & 3) * 256))
    const unsigned guo = (unsigned)((wv * 24 + ((td >> 4) & 3) * 6) * (F * 2) + gcs * 256 + (td & 15) * 16);
#define SLOT(n)                                                                                             \
  do {                                                                                                      \
    constexpr int n_ = (n);                                                                                 \
    constexpr int kind = n_ < 6 ? (n_ < 3 ? 0 : 2) : n_ < 12 ? ((n_ & 1) ? 2 : 0) : n_ < 22 ? 0 : n_ < 38 ? 1 : 3; \
    constexpr int ix = n_ < 6 ? n_ % 3 : n_ < 12 ? n_ / 2 : n_ < 22 ? n_ - 6 : n_ < 38 ? n_ - 22 : n_ - 38;   \
    if (kind == 0) Kr[ix] = ld16<KV_AUX>(rK, ko, KSO(ix));                        \
    else if (kind == 1) Vr[ix] = ld16<KV_AUX>(rV, vo, VSO(ix));                   \
    else if (kind == 2) G[ix % 6] = ld16<W1_AUX>(rgu, guo, ix * F * 2);                                     \
    else TLOAD((ix + 2) % 5);                                                                                     \
    FENCE();                                                                                                \
  } while (0)

#define TSLOT(n)                                                                                            \
  do {                                                                                                      \
    constexpr unsigned CN = TA + ((n) < 38 ? (n) * TD : 37 * TD + ((n) - 37) * TDT) / 16;                   \
    if ((n) > WIN && (n) <= 30) tref = (unsigned)__builtin_amdgcn_readfirstlane((int)tlds);                 \
    if (tprev - tref < CN) {                                                                                \
      _Pragma("nounroll") for (int spin = 0; spin < 4096; ++spin) {                                         \
        if ((unsigned)__builtin_amdgcn_s_memrealtime() - tref >= CN) break;                                 \
        __builtin_amdgcn_s_sleep(1);                                                                        \
      }                                                                                                     \
    }                                                                                                       \
    SB();                                                                                                   \
    SLOT(n);                                                                                                \
    tprev = (unsigned)__builtin_amdgcn_s_memrealtime();                                                     \
    if ((n) < 30) tlds = __hip_atomic_load(&cnt[8], __ATOMIC_RELAXED, __HIP_MEMORY_SCOPE_WORKGROUP);        \
    SB();                                                                                                   \
  } while (0)

    const int qsl = td >> 3;
    v4u Wq[3];
    auto wqload = [&]() {
      const unsigned o = (unsigned)((bd >> 4) * 512 + (bd & 3) * 128 + (td & 7) * 16);
      Wq[0] = ld16<WQ_AUX>(rq, o + (unsigned)qsl * 16384u, 0);
      Wq[1] = ld16<WQ_AUX>(rq, o + (unsigned)qsl * 16384u, 8192);
      Wq[2] = ld16<WQ_AUX>(rq, o + (unsigned)qsl * 8192u, 128 * 8192);
      FENCE();
    };
    wqload();
    const unsigned xq0 = __builtin_amdgcn_raw_buffer_load_b32(rx, (qrow0 + 2 * qsl) * 2, 0, 0);
    const unsigned xq1 = __builtin_amdgcn_raw_buffer_load_b32(rx, (qrow0 + 2 * qsl) * 2, D * 2, 0);
    const unsigned xq2 = __builtin_amdgcn_raw_buffer_load_b16(rx, (qrow0 + 128 + qsl) * 2, 0, 0);
    const unsigned xq3 = __builtin_amdgcn_raw_buffer_load_b16(rx, (qrow0 + 128 + qsl) * 2, D * 2, 0);
    const v3u xgu = __builtin_amdgcn_raw_buffer_load_b96(rx, (((td >> 2) & 1) * D + gth * 192 + wv * 24 + ((td >> 4) & 3) * 6) * 2, 0, 0);
    FENCE();
    BAR();

    {
      v2f acc[8];
#pragma unroll
      for (int j = 0; j < 8; ++j) acc[j] = (v2f){0.f, 0.f};
      static_assert(WIN == 6, "window");
      common();
      pace<10>(); SLOT(3);
      pace<10>(); SLOT(4);
      pace<10>(); SLOT(5);
      fma8(acc, Wq[0], (v2f){bflo(xq0), bflo(xq1)});
      fma8(acc, Wq[1], (v2f){bfhi(xq0), bfhi(xq1)});
      fma8(acc, Wq[2], (v2f){bflo(xq2), bflo(xq3)});
      float t[8], u[4];
#pragma unroll
      for (int j = 0; j < 8; ++j) t[j] = sw32_add(acc[j].x, acc[j].y);
#pragma unroll
      for (int j = 0; j < 4; ++j) u[j] = sw16_add(t[j], t[j + 4]);
      const bool s3 = lane & 8;
      qpart[wave][lane] = (v2f){tstep<DPP_R8>(s3, u[0], u[2]), tstep<DPP_R8>(s3, u[1], u[3])};
      lds_inc(&cnt[0], lane);
    }

    v4f gacc = {0.f, 0.f, 0.f, 0.f};
    v4f oacc = {0.f, 0.f, 0.f, 0.f}, oacc2 = {0.f, 0.f, 0.f, 0.f};
    unsigned pb[16];
    float S[16], mw, l, fw;
    v4u qb[2];
    const float SC = 0.088388347648318440f * 1.4426950408889634f;
#define GU_STEP(k) gacc = mfma(andm(((k) & 1) ? duphi(xgu[((k) >> 1) % 3]) : duplo(xgu[((k) >> 1) % 3]), msk), G[(k) % 6], gacc)
#define PV_STEP(i)                                                           \
  do {                                                                       \
    if ((i) & 1) oacc2 = mfma(andm(pb[(i) % 16], msk), Vr[(i) % 16], oacc2); \
    else oacc = mfma(andm(pb[(i) % 16], msk), Vr[(i) % 16], oacc);           \
  } while (0)
    static_for<WIN, (QS + 6 > NS ? QS + 6 : NS)>([&](auto n) {
      constexpr int N = n.value;
      if constexpr (N < NS) {
        if (N < WB_POS) pace<WIN - 1>();
        else if (N < 38 || WINB >= 5) pace<WINB - 1>();
        else pace<4>();
        TSLOT(N);
      }
      if constexpr (N - WIN >= 3 && N - WIN <= 5) GU_STEP(N - WIN - 3);
      else if constexpr (N - WIN >= 7 && ((N - WIN) & 1) && (N - WIN - 1) / 2 < 6) GU_STEP((N - WIN - 1) / 2);
      if (N == 11 + WIN) {
        gured[wave][lane] = gacc;
        lds_inc(&cnt[1], lane);
      }
      if (N == QS) {
        lds_wait(&cnt[2], 1u);
#pragma unroll
        for (int j = 0; j < 2; ++j) qb[j] = *(const v4u*)&qs[((c >> 2) & 1) * 64 + j * 32 + (c >> 3) * 16 + r * 4];
        tag = (unsigned)__builtin_amdgcn_readfirstlane((int)cnt[6]);
      }
      if (N >= QS && N < QS + 4) {
        constexpr int P = (N - QS) & 3;
        v4f X = mfma(Kr[4 * P], qb[0], (v4f){0.f, 0.f, 0.f, 0.f}), Y = mfma(Kr[4 * P + 2], qb[0], (v4f){0.f, 0.f, 0.f, 0.f});
        X = mfma(Kr[4 * P + 1], qb[1], X);
        Y = mfma(Kr[4 * P + 3], qb[1], Y);
#pragma unroll
        for (int t = 0; t < 4; ++t) {
          const float xv = X[t], yv = Y[t];
          auto sw = __builtin_amdgcn_permlane32_swap(asu(xv), asu(yv), false, false);
          const float u = (c & 8) ? asf((unsigned)sw[1]) : asf((unsigned)sw[0]);
          S[4 * P + t] = u + dpp<DPP_R8>(u);
        }
        FENCE();
      }
      if (N == QS + 4) {
        float m = fmaxf(fmaxf(fmaxf(S[0], S[1]), fmaxf(S[2], S[3])), fmaxf(fmaxf(S[4], S[5]), fmaxf(S[6], S[7])));
        m = fmaxf(m, fmaxf(fmaxf(fmaxf(S[8], S[9]), fmaxf(S[10], S[11])), fmaxf(fmaxf(S[12], S[13]), fmaxf(S[14], S[15]))));
        m = sw32_max(sw16_max(m));
        mw = m * SC;
        l = 0.f;
      }
      if (N == QS + 4 || N == QS + 5) {
#pragma unroll
        for (int i = (N - QS - 4) * 8; i < (N - QS - 3) * 8; ++i) {
          const float p = __builtin_amdgcn_exp2f(__builtin_fmaf(S[i & 15], SC, -mw));
          pb[i & 15] = pack2(p, p);
          l += bflo(pb[i & 15]);
        }
        FENCE();
      }
      if (N == QS + 5) {
        l = sw16_add(l, l);
        const float lw = sw32_add(l, l);
        if ((lane & 59) == 0) { mls[0][lane >> 2][wave] = mw; mls[1][lane >> 2][wave] = lw; }
        lds_inc(&cnt[7], lane);
      }
      if (N == FS) {
        lds_wait(&cnt[7], 8u);
        const v4f ma = *(const v4f*)&mls[0][r & 1][0], mb = *(const v4f*)&mls[0][r & 1][4];
        const float mo = mls[0][r & 1][wave];
        const float M = fmaxf(fmaxf(fmaxf(ma[0], ma[1]), fmaxf(ma[2], ma[3])), fmaxf(fmaxf(mb[0], mb[1]), fmaxf(mb[2], mb[3])));
        fw = __builtin_amdgcn_exp2f(mo - M);
        FENCE();
      }
      if constexpr (pv_done(N) > pv_done(N - 1)) {
        static_for<pv_done(N - 1), pv_done(N)>([&](auto i) { PV_STEP(i.value); });
        FENCE();
      }
    });
    static_for<pv_done((QS + 6 > NS ? QS + 6 : NS) - 1), 16>([&](auto i) { PV_STEP(i.value); });
    dred[wave][lane] = (oacc + oacc2) * fw;
    const unsigned rank = rank_of(lds_add(&cnt[3], lane));
    if (rank == 7u) {
      v4f dv[8];
#pragma unroll
      for (int w = 0; w < 8; ++w) dv[w] = dred[w][lane];
      const v4f o = ((dv[0] + dv[1]) + (dv[2] + dv[3])) + ((dv[4] + dv[5]) + (dv[6] + dv[7]));
      const float o0 = o[0], o1 = o[1], o2 = o[2], o3 = o[3];
      auto e = __builtin_amdgcn_permlane16_swap(asu(o0), asu(o2), false, false);
      auto f = __builtin_amdgcn_permlane16_swap(asu(o1), asu(o3), false, false);
      const unsigned pa = WS_P + (unsigned)(((kvh * 16 + (c >> 1)) * 16 + i16) * 128 + ((c & 1) * 4 + r) * 16);
      st16(rws, pa, (v4u){(unsigned)e[0], tag, (unsigned)f[0], tag});
      st16(rws, pa + 8u * 16u * 128u, (v4u){(unsigned)e[1], tag, (unsigned)f[1], tag});
    }
    FENCE();
    static_for<NS, 43>([&](auto n) { TSLOT(n.value); });
    lds_wait(&cnt[5], 1u);
  }
  const unsigned* cp = &cf[(c >> 2) & 1][r][0];
  v4f od;
  float gx;
  {
    const v4u cb = *(const v4u*)(cp + 4);
    const unsigned cc = cp[8];
    const v2f gg = gs[r & 1];
    v4f X = mfma(andm(cb[0], msk), T[2], (v4f){0.f, 0.f, 0.f, 0.f}), Y = mfma(andm(cb[1], msk), T[2], (v4f){0.f, 0.f, 0.f, 0.f});
    X = mfma(andm(cb[2], msk), T[3], X);
    Y = mfma(andm(cb[3], msk), T[3], Y);
    X = mfma(andm(cc, msk), T[4], X);
    od = ((c & 8) ? Y : X) * gg.y;
    gx = gg.x;
  }
  unsigned oaddr = WS_O + (unsigned)(wave * 4 + (c >> 1)) * 32768u + (unsigned)(g * 128 + (c & 1) * 64 + r * 16);
  asm volatile("" : "+v"(T[0]), "+v"(T[1]), "+v"(od), "+v"(gx), "+v"(oaddr));
  if (wave == 8) {
    const unsigned poff = WS_P + (unsigned)(g * 2048 + lane * 16);
    const unsigned hoff[2] = {poff, poff + 1024u};
    v4u hg[2];
    unsigned hv[4];
    const unsigned moff[2] = {WS_M + (unsigned)((kvh * 16 + (lane >> 3)) * 128 + (i16 >> 3) * 16),
                              WS_M + (unsigned)((kvh * 16 + 8 + (lane >> 3)) * 128 + (i16 >> 3) * 16)};
    v4u mg[2];
    twait(tref, TM);
#pragma unroll
    for (int k = 0; k < 2; ++k) mg[k] = ld16<AUX_SC1>(rws, moff[k], 0);
    FENCE();
    lds_wait(&cnt[3], 8u);
#pragma unroll
    for (int k = 0; k < 2; ++k) hg[k] = ld16<AUX_SC1>(rws, hoff[k], 0);
    FENCE();
    float pw[2];
    poll<2>(rws, moff, mg, tag);
#pragma unroll
    for (int k = 0; k < 2; ++k) {
      const float M = asf(mg[k][0]);
      const float Mx = sw32_max(sw16_max(fmaxf(M, dpp<DPP_R8>(M))));
      const float f = __builtin_amdgcn_exp2f(M - Mx);
      float L = f * asf(mg[k][2]);
      L += dpp<DPP_R8>(L); L = sw16_add(L, L); L = sw32_add(L, L);
      pw[k] = f / L;
    }
    unsigned cw = (unsigned)(((lane >> 5) * 48 + ((lane & 1) * 2 + ((lane >> 4) & 1)) * 12 + ((lane & 7) >> 1)) * 4);
    asm volatile("" : "+v"(pw[0]), "+v"(pw[1]), "+v"(cw));
    poll2<2>(rwsv, hoff, hg, hv, tag);
    float v = sw16_add(sw32_add(pw[0] * asf(hv[0]), pw[1] * asf(hv[2])), sw32_add(pw[0] * asf(hv[1]), pw[1] * asf(hv[3])));
    v += dpp<DPP_R8>(v);
    if (!(lane & 8)) *(unsigned*)((char*)cf + cw) = pack2(v, v);
  }
  BAR();
  {
    const v4u ca = *(const v4u*)cp;
    v4f X = mfma(andm(ca[0], msk), T[0], (v4f){0.f, 0.f, 0.f, 0.f}), Y = mfma(andm(ca[1], msk), T[0], (v4f){0.f, 0.f, 0.f, 0.f});
    X = mfma(andm(ca[2], msk), T[1], X);
    Y = mfma(andm(ca[3], msk), T[1], Y);
    v4f o = ((c & 8) ? Y : X) * gx + od;
#pragma unroll
    for (int t = 0; t < 4; ++t) { const float v = o[t]; o[t] = v + dpp<DPP_R8>(v); }
    if (c < 8) st16(rws, oaddr, (v4u){pack2(o[0], o[1]), tag, pack2(o[2], o[3]), tag});
  }
  FENCE();
  if (bid < 72 && wave < 8) {
    unsigned ooff[2], ov[4];
    v4u og[2];
    __builtin_amdgcn_s_sleep(OWAIT);
#pragma unroll
    for (int k = 0; k < 2; ++k) {
      ooff[k] = WS_O + (unsigned)((bid >> 1) * 32768 + (bid & 1) * 64 + (wave * 32 + k * 16 + (lane >> 2)) * 128 + (lane & 3) * 16);
      og[k] = ld16<AUX_SC1>(rws, ooff[k], 0);
    }
    poll2<2>(rwsv, ooff, og, ov, tag);
    const float a0 = bflo(ov[0]) + bflo(ov[2]), a1 = bfhi(ov[0]) + bfhi(ov[2]), a2 = bflo(ov[1]) + bflo(ov[3]), a3 = bfhi(ov[1]) + bfhi(ov[3]);
    float u0 = sw16_add(sw32_add(a0, a2), sw32_add(a1, a3));
    u0 += dpp<DPP_R4>(u0); u0 += dpp<DPP_R8>(u0);
    if (c < 4) opart[c * 4 + r][wave] = u0;
    if (rank_of(lds_add(&cnt[4], lane)) == 7u && lane < 16) {
      const v4f pa = *(const v4f*)&opart[lane][0], pb2 = *(const v4f*)&opart[lane][4];
      const float tot = ((pa[0] + pa[1]) + (pa[2] + pa[3])) + ((pb2[0] + pb2[1]) + (pb2[2] + pb2[3]));
      out[((lane >> 2) & 1) * D + bid * 8 + (lane >> 3) * 4 + (lane & 3)] = (u16)f2bf(tot);
      if (bid == 0 && lane == 0) __builtin_amdgcn_raw_buffer_store_b32(tag, rws, 0, 0, AUX_SC1);
    }
  }
}

extern "C" void kernel_launch(void* const* d_in, const int* in_sizes, int n_in, void* d_out, int out_size, void* d_ws, size_t ws_size,
                              hipStream_t stream) {
  fused_decode<<<256, 576, 0, stream>>>((const u16*)d_in[0], (const u16*)d_in[1], (const u16*)d_in[2], (const u16*)d_in[3],
                                        (const u16*)d_in[4], (const u16*)d_in[6], (const u16*)d_in[7], (const u16*)d_in[8],
                                        (const u16*)d_in[9], (u16*)d_out, (unsigned char*)d_ws);
}
```
